```python
import torch

def _softmax(x, axis=-1):
    return torch.softmax(x, dim=axis)
def _gelu(x):
    return torch.nn.functional.gelu(x, approximate="tanh")
def _silu(x):
    return torch.nn.functional.silu(x)
def _sigmoid(x):
    return torch.sigmoid(x)
def _layernorm(x, axis=-1, eps=1e-5):
    m = x.mean(dim=axis, keepdim=True)
    v = x.var(dim=axis, keepdim=True, unbiased=False)
    return (x - m) / torch.sqrt(v + eps)
def _scatter_add(tgt, idx, src, axis=0):
    return tgt.clone().index_add_(axis, idx, src)
def _segment_sum(src, seg, n):
    out = torch.zeros((n,) + src.shape[1:], dtype=src.dtype)
    return out.index_add_(0, seg, src)
def _pad(x, pw):
    flat = tuple(v for pair in reversed(pw) for v in pair)
    return torch.nn.functional.pad(x, flat)

kernel_name = 'standalone__gqa_softmax__sampling'

def setup_inputs():

    _w = (lambda a: (lambda t: t.to(torch.bfloat16) if t.is_floating_point() else t)(torch.as_tensor(a)))
    B, Hq, Hkv, Sq, Sk, D = 8, 32, 8, 1, 8192, 128
    return {
        "q": _w((lambda *s: torch.randn(*s, dtype=torch.bfloat16) * 0.8)(B, Hq, Sq, D)),
        "k": _w((lambda *s: torch.randn(*s, dtype=torch.bfloat16) * 0.8)(B, Hkv, Sk, D)),
        "v": _w((lambda *s: torch.randn(*s, dtype=torch.bfloat16) * 0.05)(B, Hkv, Sk, D)),
    }

def reference(q, k, v):
    B, Hq, Sq, D = q.shape
    Hkv = k.shape[1]
    rep = Hq // Hkv
    k = (lambda x, n, axis: torch.repeat_interleave(x, n, dim=axis))(k, rep, axis=1)
    v = (lambda x, n, axis: torch.repeat_interleave(x, n, dim=axis))(v, rep, axis=1)
    scale = D ** -0.5
    scores = torch.einsum("bhqd,bhkd->bhqk", q, k) * scale
    p = _softmax(scores, axis=-1)
    return torch.einsum("bhqk,bhkd->bhqd", p, v)
```

```cpp
#include <hip/hip_runtime.h>
#include <hip/hip_bf16.h>
#include <cstdint>

#define NSPLIT 4
#define WAVES 4
#define DEPTH 1
#define LAG 3
#define KAUX 2
#define VAUX 2

#define GRIDN (B * HKV * NSPLIT)
constexpr int B = 8, HQ = 32, HKV = 8, G = 4, SK = 8192, D = 128;
constexpr int NTHR = WAVES * 64;
constexpr int TOK_PER_WG = SK / NSPLIT;
constexpr int TILES_PER_WAVE = TOK_PER_WG / (16 * WAVES);
static_assert(TILES_PER_WAVE % DEPTH == 0, "ring");
constexpr float C2 = 0.088388347648318440f * 1.4426950408889634f;

using u32x4 = __attribute__((ext_vector_type(4))) unsigned;
using u32x2 = __attribute__((ext_vector_type(2))) unsigned;
using f32x4 = __attribute__((ext_vector_type(4))) float;
using bf16x8 = __attribute__((ext_vector_type(8))) __bf16;
using f32x2 = __attribute__((ext_vector_type(2))) float;
using bf16x2 = __attribute__((ext_vector_type(2))) __bf16;
using s16x4 = __attribute__((ext_vector_type(4))) short;

__device__ __forceinline__ unsigned cvtpk(float lo, float hi) {
  const f32x2 f = {lo, hi};
  return __builtin_bit_cast(unsigned, __builtin_convertvector(f, bf16x2));
}
__device__ __forceinline__ float xrow_max(float x) {
  auto s = __builtin_amdgcn_permlane16_swap(__float_as_uint(x), __float_as_uint(x), false, false);
  x = fmaxf(__uint_as_float(s[0]), __uint_as_float(s[1]));
  auto t = __builtin_amdgcn_permlane32_swap(__float_as_uint(x), __float_as_uint(x), false, false);
  return fmaxf(__uint_as_float(t[0]), __uint_as_float(t[1]));
}
__device__ __forceinline__ float xrow_sum(float x) {
  auto s = __builtin_amdgcn_permlane16_swap(__float_as_uint(x), __float_as_uint(x), false, false);
  x = __uint_as_float(s[0]) + __uint_as_float(s[1]);
  auto t = __builtin_amdgcn_permlane32_swap(__float_as_uint(x), __float_as_uint(x), false, false);
  return __uint_as_float(t[0]) + __uint_as_float(t[1]);
}

template <int CTRL>
__device__ __forceinline__ float qbc(float x) {
  return __uint_as_float(__builtin_amdgcn_mov_dpp(__float_as_uint(x), CTRL, 0xf, 0xf, true));
}

constexpr int REC = 4 + D;

typedef __attribute__((address_space(1))) unsigned long long gu64;
#define RLX_AGENT __ATOMIC_RELAXED, __HIP_MEMORY_SCOPE_AGENT
constexpr int CTL_STRIDE = 256;
constexpr int CTL_BYTES = B * HKV * CTL_STRIDE;
constexpr int GREC = 1152;
constexpr int GRP_BYTES = NSPLIT * G * GREC;

__global__ void __launch_bounds__(NTHR, 2) attn_split(const unsigned char* __restrict__ q, const unsigned char* __restrict__ k,
                                                   const unsigned char* __restrict__ v, unsigned char* ws, unsigned* __restrict__ out) {
  __shared__ __attribute__((aligned(16))) float lds[WAVES * G * REC];
  const int bid = blockIdx.x;
  const int bh = (unsigned)bid / NSPLIT, s = (unsigned)bid % NSPLIT;
  const int tid = threadIdx.x, lane = tid & 63, wave = __builtin_amdgcn_readfirstlane(tid >> 6);
  const int hi = lane >> 4, lo = lane & 15;
  constexpr int SLICE = TOK_PER_WG * D * 2;
  void* const kb = (void*)(k + (size_t)bid * SLICE);
  void* const vb = (void*)(v + (size_t)bid * SLICE);
  const auto kr = __builtin_amdgcn_make_buffer_rsrc(kb, 0, SLICE, 0x00020000);
  const auto vr = __builtin_amdgcn_make_buffer_rsrc(vb, 0, SLICE, 0x00020000);
  const int koff = (lo >> 2) * 256 + hi * 64 + (lo & 3) * 16;
  const int voff = lane * 16;
  constexpr int TSTRIDE = WAVES * 16 * 256;
  int soff = wave * 4096;
  gu64* const ctl = (gu64*)(ws + bh * CTL_STRIDE);
  u32x4 cw = __builtin_amdgcn_raw_buffer_load_b128(__builtin_amdgcn_make_buffer_rsrc((void*)ctl, 0, 16, 0x00020000), 0, 0, 16);
  const u32x4 qf = *reinterpret_cast<const u32x4*>(q + (size_t)(bh * G + (lo >> 2)) * (D * 2) + hi * 64 + (lo & 3) * 16);
  constexpr int RING = 8 * DEPTH;
  u32x4 R[RING];
  auto fill = [&](int slot, decltype(kr) ks, decltype(kr) vs, int so) {
    const int d = slot >> 3, i = slot & 7;
    R[slot] = i < 4 ? __builtin_amdgcn_raw_buffer_load_b128(ks, koff + i * 1024, so + d * TSTRIDE, KAUX)
                    : __builtin_amdgcn_raw_buffer_load_b128(vs, voff + (i - 4) * 1024, so + d * TSTRIDE, VAUX);
  };
#pragma unroll
  for (int i = 0; i < RING - LAG; ++i) fill(i, kr, vr, soff);
  __builtin_amdgcn_sched_barrier(0);
  asm volatile("" : "+v"(cw));
  const unsigned long long tag = (((unsigned long long)cw[1] << 32) | cw[0]) + 1;
  if (tid == 0) {
    unsigned long long tick = ((unsigned long long)cw[3] << 32) | cw[2];
    if ((tick >> 16) != (tag & 0xffffffffffffull))
      __hip_atomic_compare_exchange_strong(ctl + 1, &tick, tag << 16, __ATOMIC_RELAXED, RLX_AGENT);
  }
  const int jj = lo & 3;
  const unsigned mask0 = jj == 0 ? 0x0000ffffu : (jj == 1 ? 0xffff0000u : 0u);
  const unsigned mask1 = jj == 2 ? 0x0000ffffu : (jj == 3 ? 0xffff0000u : 0u);

  float m = -INFINITY, l = 0.f;
  f32x4 acc0 = {0.f, 0.f, 0.f, 0.f}, acc1 = {0.f, 0.f, 0.f, 0.f};
#pragma unroll 1
  for (int it = 0; it < TILES_PER_WAVE / DEPTH; ++it) {
    const int nrec = it + 1 < TILES_PER_WAVE / DEPTH ? SLICE : 0;
    const auto kn = __builtin_amdgcn_make_buffer_rsrc(kb, 0, nrec, 0x00020000);
    const auto vn = __builtin_amdgcn_make_buffer_rsrc(vb, 0, nrec, 0x00020000);
    auto refill = [&](int p) {
      if (p >= LAG) fill(p - LAG, kn, vn, soff + DEPTH * TSTRIDE); else fill(p - LAG + RING, kr, vr, soff);
      __builtin_amdgcn_sched_barrier(0);
    };
#pragma unroll
    for (int d = 0; d < DEPTH; ++d) {
      f32x4 dd[4];
#pragma unroll
      for (int i = 0; i < 4; ++i) {
        dd[i] = __builtin_amdgcn_mfma_f32_16x16x32_bf16(__builtin_bit_cast(bf16x8, R[8 * d + i]), __builtin_bit_cast(bf16x8, qf), f32x4{0.f, 0.f, 0.f, 0.f}, 0, 0, 0);
        asm volatile("" :: "v"(dd[i]) : "memory");
        refill(8 * d + i);
      }
      float sc[4];
#pragma unroll
      for (int i = 0; i < 4; ++i) sc[i] = (qbc<0x00>(dd[i][0]) + qbc<0x55>(dd[i][1])) + (qbc<0xaa>(dd[i][2]) + qbc<0xff>(dd[i][3]));
      const float lmax = fmaxf(fmaxf(sc[0], sc[1]), fmaxf(sc[2], sc[3]));
      if (__any(lmax > m)) {
        const float mn = fmaxf(m, xrow_max(lmax));
        const float alpha = __builtin_amdgcn_exp2f((m - mn) * C2);
        l *= alpha;
#pragma unroll
        for (int e = 0; e < 4; ++e) { acc0[e] *= alpha; acc1[e] *= alpha; }
        m = mn;
      }
      const float mc = m * C2;
      u32x2 bop[4];
#pragma unroll
      for (int r = 0; r < 4; ++r) {
        const float p = __builtin_amdgcn_exp2f(fmaf(sc[r], C2, -mc));
        l += p;
        const unsigned pb = cvtpk(p, p);
        bop[r] = u32x2{pb & mask0, pb & mask1};
      }
      __builtin_amdgcn_sched_barrier(0);
#pragma unroll
      for (int r = 0; r < 4; ++r) {
        const u32x4 vv = R[8 * d + 4 + r];
        const u32x2 a0 = {vv[0], vv[1]}, a1 = {vv[2], vv[3]};
        acc0 = __builtin_amdgcn_mfma_f32_16x16x16bf16_1k(__builtin_bit_cast(s16x4, a0), __builtin_bit_cast(s16x4, bop[r]), acc0, 0, 0, 0);
        acc1 = __builtin_amdgcn_mfma_f32_16x16x16bf16_1k(__builtin_bit_cast(s16x4, a1), __builtin_bit_cast(s16x4, bop[r]), acc1, 0, 0, 0);
        asm volatile("" :: "v"(acc0), "v"(acc1) : "memory");
        refill(8 * d + 4 + r);
      }
    }
    soff += DEPTH * TSTRIDE;
  }
  l = xrow_sum(l);
  {
    float* rec = lds + (wave * G + (lo >> 2)) * REC;
#pragma unroll
    for (int e = 0; e < 4; ++e) { rec[4 + 8 * (4 * hi + e) + jj] = acc0[e]; rec[4 + 8 * (4 * hi + e) + 4 + jj] = acc1[e]; }
    if (hi == 0 && jj == 0) { rec[0] = m; rec[1] = l; }
  }
  asm volatile("s_waitcnt vmcnt(0)" ::: "memory");
  __syncthreads();
  if (wave < G) {
    const int g = wave, d0 = 2 * lane;
    unsigned old; unsigned long long keep;
    asm volatile("s_mov_b64 %1, exec\n\ts_mov_b64 exec, 1\n\tglobal_atomic_add %0, %2, %3, %4 offset:8 sc0\n\ts_mov_b64 exec, %1"
                 : "=&v"(old), "=&s"(keep) : "v"(0), "v"(1u << (4 * g)), "s"(ctl) : "memory");
    const auto gr = __builtin_amdgcn_make_buffer_rsrc((void*)(ws + CTL_BYTES + (size_t)bh * GRP_BYTES), 0, GRP_BYTES, 0x00020000);
    u32x4 ro[NSPLIT], rm[NSPLIT];
#pragma unroll
    for (int i = 0; i < NSPLIT; ++i) {
      ro[i] = __builtin_amdgcn_raw_buffer_load_b128(gr, lane * 16, (i * G + g) * GREC, 16);
      rm[i] = __builtin_amdgcn_raw_buffer_load_b128(gr, 1024, (i * G + g) * GREC, 16);
    }
    const unsigned t32 = (unsigned)tag;
    float M = -INFINITY;
#pragma unroll
    for (int w = 0; w < WAVES; ++w) M = fmaxf(M, lds[(w * G + g) * REC]);
    float L = 0.f, o0 = 0.f, o1 = 0.f;
#pragma unroll
    for (int w = 0; w < WAVES; ++w) {
      const float* rec = lds + (w * G + g) * REC;
      const float wt = __builtin_amdgcn_exp2f((rec[0] - M) * C2);
      const float2 a = *reinterpret_cast<const float2*>(rec + 4 + d0);
      L = fmaf(wt, rec[1], L); o0 = fmaf(wt, a.x, o0); o1 = fmaf(wt, a.y, o1);
    }
    const u32x4 po = {__float_as_uint(o0), t32, __float_as_uint(o1), t32};
    const u32x4 pm = {__float_as_uint(M), t32, __float_as_uint(L), t32};
    __builtin_amdgcn_raw_buffer_store_b128(po, gr, lane * 16, (s * G + g) * GREC, 16);
    __builtin_amdgcn_raw_buffer_store_b128(pm, gr, lane == 0 ? 1024 : GRP_BYTES, (s * G + g) * GREC, 16);
    asm volatile("s_waitcnt vmcnt(%1)" : "+v"(old) : "i"(2 * NSPLIT + 2) : "memory");
    const unsigned cnt = (__builtin_amdgcn_readfirstlane(old) >> (4 * g)) & 15u;
    if (cnt != NSPLIT - 1) return;
    auto fresh = [&]() {
      unsigned x = 0;
#pragma unroll
      for (int i = 0; i < NSPLIT; ++i) {
        if (i == s) { ro[i] = po; rm[i] = pm; }
        x |= (ro[i][1] ^ t32) | (ro[i][3] ^ t32) | (rm[i][1] ^ t32) | (rm[i][3] ^ t32);
      }
      return __all(x == 0);
    };
    if (__builtin_expect(!fresh(), 0)) {
      int spins = 0;
      do {
        asm volatile("" ::: "memory");
#pragma unroll
        for (int i = 0; i < NSPLIT; ++i) {
          ro[i] = __builtin_amdgcn_raw_buffer_load_b128(gr, lane * 16, (i * G + g) * GREC, 16);
          rm[i] = __builtin_amdgcn_raw_buffer_load_b128(gr, 1024, (i * G + g) * GREC, 16);
        }
      } while (!fresh() && ++spins < (1 << 20));
    }
    M = -INFINITY;
#pragma unroll
    for (int i = 0; i < NSPLIT; ++i) M = fmaxf(M, __uint_as_float(rm[i][0]));
    L = 0.f; o0 = 0.f; o1 = 0.f;
#pragma unroll
    for (int i = 0; i < NSPLIT; ++i) {
      const float wt = __builtin_amdgcn_exp2f((__uint_as_float(rm[i][0]) - M) * C2);
      L = fmaf(wt, __uint_as_float(rm[i][2]), L);
      o0 = fmaf(wt, __uint_as_float(ro[i][0]), o0); o1 = fmaf(wt, __uint_as_float(ro[i][2]), o1);
    }
    const float inv = 1.f / L;
    out[(bh * G + g) * (D / 2) + lane] = cvtpk(o0 * inv, o1 * inv);
    if (g == 0 && lane == 0) __hip_atomic_store(ctl, tag, RLX_AGENT);
  }
}

extern "C" void kernel_launch(void* const* d_in, const int* in_sizes, int n_in, void* d_out, int out_size, void* d_ws, size_t ws_size,
                              hipStream_t stream) {
  hipLaunchKernelGGL(attn_split, dim3(GRIDN), dim3(NTHR), 0, stream, (const unsigned char*)d_in[0], (const unsigned char*)d_in[1],
                     (const unsigned char*)d_in[2], (unsigned char*)d_ws, (unsigned*)d_out);
}
```

```cpp
#include <hip/hip_runtime.h>
#include <hip/hip_bf16.h>
#include <cstdint>

#define NSPLIT 4
#define WAVES 4
#define MAP 0
#define KAUX 18
#define VAUX 18

#define PACE 1
#define NC8 32
#define GAIN 40
#define NMIN 2
#define TPT8 852
#define OFF 130
#define SPRINT 29
#define SKEW 10
#define LEAD 40
#define JUMP 100
#define LAGCAP 1000
#define PT 1
#define PTI 30
#define CB 4
#define GRIDN (B * HKV * NSPLIT)
constexpr int B = 8, HQ = 32, HKV = 8, G = 4, SK = 8192, D = 128;
constexpr int NTHR = WAVES * 64;
constexpr int TOK_PER_WG = SK / NSPLIT;
constexpr int TILES_PER_WAVE = TOK_PER_WG / (16 * WAVES);
constexpr float C2 = 0.088388347648318440f * 1.4426950408889634f;

using u32x4 = __attribute__((ext_vector_type(4))) unsigned;
using u32x2 = __attribute__((ext_vector_type(2))) unsigned;
using f32x4 = __attribute__((ext_vector_type(4))) float;
using bf16x8 = __attribute__((ext_vector_type(8))) __bf16;
using f32x2 = __attribute__((ext_vector_type(2))) float;
using bf16x2 = __attribute__((ext_vector_type(2))) __bf16;
using s16x4 = __attribute__((ext_vector_type(4))) short;

__device__ __forceinline__ unsigned cvtpk(float lo, float hi) {
  const f32x2 f = {lo, hi};
  return __builtin_bit_cast(unsigned, __builtin_convertvector(f, bf16x2));
}
__device__ __forceinline__ float xrow_max(float x) {
  auto s = __builtin_amdgcn_permlane16_swap(__float_as_uint(x), __float_as_uint(x), false, false);
  x = fmaxf(__uint_as_float(s[0]), __uint_as_float(s[1]));
  auto t = __builtin_amdgcn_permlane32_swap(__float_as_uint(x), __float_as_uint(x), false, false);
  return fmaxf(__uint_as_float(t[0]), __uint_as_float(t[1]));
}
__device__ __forceinline__ float xrow_sum(float x) {
  auto s = __builtin_amdgcn_permlane16_swap(__float_as_uint(x), __float_as_uint(x), false, false);
  x = __uint_as_float(s[0]) + __uint_as_float(s[1]);
  auto t = __builtin_amdgcn_permlane32_swap(__float_as_uint(x), __float_as_uint(x), false, false);
  return __uint_as_float(t[0]) + __uint_as_float(t[1]);
}

template <int CTRL>
__device__ __forceinline__ float qbc(float x) {
  return __uint_as_float(__builtin_amdgcn_mov_dpp(__float_as_uint(x), CTRL, 0xf, 0xf, true));
}

template <int P>
__device__ __forceinline__ void throttle(int lag) {
  constexpr int ph = ((P & 1) << 2) | (P & 2) | ((P & 4) >> 2);
#define THR(k) ((8 * (k) - NC8 - ph) * GAIN / 8)
  asm volatile("s_cmp_ge_i32 %0, %1\n\ts_cbranch_scc1 1f\n\ts_waitcnt vmcnt(6)\n\t"
               "s_cmp_ge_i32 %0, %2\n\ts_cbranch_scc1 1f\n\ts_waitcnt vmcnt(5)\n\t"
               "s_cmp_ge_i32 %0, %3\n\ts_cbranch_scc1 1f\n\ts_waitcnt vmcnt(4)\n\t"
               "s_cmp_ge_i32 %0, %4\n\ts_cbranch_scc1 1f\n\ts_waitcnt vmcnt(3)\n\t"
               "s_cmp_ge_i32 %0, %5\n\ts_cbranch_scc1 1f\n\ts_waitcnt vmcnt(2)\n\t"
               "1:" :: "s"(lag), "i"(THR(7)), "i"(THR(6)), "i"(THR(5)), "i"(THR(4)), "i"(THR(3)) : "scc", "memory");
#undef THR
}

constexpr int REC = 4 + D;

typedef __attribute__((address_space(1))) unsigned gu32;
#define RLX_AGENT __ATOMIC_RELAXED, __HIP_MEMORY_SCOPE_AGENT
constexpr int CTL_STRIDE = 256;
constexpr int CTL_BYTES = B * HKV * CTL_STRIDE;
constexpr int HREC = 4096;
constexpr int HUSED = 3072 + 3 * 128;
constexpr int GRP_BYTES = G * HREC;
constexpr int NT = TILES_PER_WAVE;
constexpr int TPT_N = TPT8 * (3 * (NT - 1) + SPRINT) / (4 * (NT - 1));
constexpr int TPT_O = (TPT_N * (NT - 1) - 8 * SKEW) / SPRINT;
static_assert(NSPLIT == 4 && WAVES == G, "layout");

__global__ void __launch_bounds__(NTHR, 2) attn_split(const unsigned char* __restrict__ q, const unsigned char* __restrict__ k,
                                                   const unsigned char* __restrict__ v, unsigned char* ws, unsigned* __restrict__ out) {
  __shared__ __attribute__((aligned(16))) float lds[WAVES * G * REC];
  const int bid = MAP == 0 ? blockIdx.x : (blockIdx.x & 7) * (GRIDN / 8) + (blockIdx.x >> 3);
  const int bh = (unsigned)bid / NSPLIT, s = (unsigned)bid % NSPLIT;
  const int tid = threadIdx.x, lane = tid & 63, wave = __builtin_amdgcn_readfirstlane(tid >> 6);
  const int hi = lane >> 4, lo = lane & 15;
  constexpr int SLICE = (32 * 4096) << CB;
  const int W = bid * WAVES + wave;
  void* const kb = (void*)(k + (size_t)(W >> CB) * SLICE);
  void* const vb = (void*)(v + (size_t)(W >> CB) * SLICE);
  const auto kr = __builtin_amdgcn_make_buffer_rsrc(kb, 0, SLICE, 0x00020000);
  const auto vr = __builtin_amdgcn_make_buffer_rsrc(vb, 0, SLICE, 0x00020000);
  const int koff = (lo >> 2) * 256 + hi * 64 + (lo & 3) * 16;
  const int voff = lane * 16;
  constexpr int TSTRIDE = 4096 << CB;
  int soff = (W & ((1 << CB) - 1)) * 4096;
  gu32* const ctl = (gu32*)(ws + bh * CTL_STRIDE);
  unsigned cw = __builtin_amdgcn_raw_buffer_load_b32(__builtin_amdgcn_make_buffer_rsrc((void*)ctl, 0, 4, 0x00020000), 0, 0, 16);
  const u32x4 qf = *reinterpret_cast<const u32x4*>(q + (size_t)(bh * G + (lo >> 2)) * (D * 2) + hi * 64 + (lo & 3) * 16);
  u32x4 R[8];
  auto fill = [&](int i, decltype(kr) ks, decltype(kr) vs, int ko, int so) {
    R[i] = i < 4 ? __builtin_amdgcn_raw_buffer_load_b128(ks, ko + i * 1024, so, KAUX)
                 : __builtin_amdgcn_raw_buffer_load_b128(vs, voff + (i - 4) * 1024, so, VAUX);
  };
#pragma unroll
  for (int i = 0; i < 8; ++i) fill(i, kr, vr, koff, soff);
  __builtin_amdgcn_sched_barrier(0);
  const unsigned t_beg = (unsigned)__builtin_amdgcn_s_memrealtime();
  const int jj = lo & 3;
  const unsigned mask0 = jj == 0 ? 0x0000ffffu : (jj == 1 ? 0xffff0000u : 0u);
  const unsigned mask1 = jj == 2 ? 0x0000ffffu : (jj == 3 ? 0xffff0000u : 0u);

  const int rank = (s - (bh >> 1) - 1) & 3;
  const bool own = rank == 3;
  void* const gb = (void*)(ws + CTL_BYTES + (size_t)bh * GRP_BYTES + wave * HREC);
  const auto gr = __builtin_amdgcn_make_buffer_rsrc(gb, 0, HUSED, 0x00020000);
  const int grec = own ? HUSED : 0;
  const auto pr = __builtin_amdgcn_make_buffer_rsrc(own ? (void*)(out + (bh * G + wave) * (D / 2)) : (void*)((char*)gb + 3584), 0,
                                                    own ? ((PT & 3) ? 4 : 0) : ((PT & 4) ? 4 : 0), 0x00020000);
  unsigned px = 0;
  if (PT & 1) px = __builtin_amdgcn_raw_buffer_load_b32(pr, voff, 0, 16);

  const int tpt = own ? TPT_O : TPT_N, fast = own ? SPRINT : NT - 1;
  unsigned ref = t_beg + OFF, prev = ref;
  int lag = 0;
  float m = -INFINITY, l = 0.f;
  f32x4 acc0 = {0.f, 0.f, 0.f, 0.f}, acc1 = {0.f, 0.f, 0.f, 0.f};
#pragma unroll 1
  for (int it = 0; it < NT; ++it) {
    const bool more = it + 1 < NT;
    soff += TSTRIDE;
    const auto kn = __builtin_amdgcn_make_buffer_rsrc(more ? kb : gb, 0, more ? SLICE : grec, 0x00020000);
    const auto vn = __builtin_amdgcn_make_buffer_rsrc(more ? vb : gb, 0, more ? SLICE : grec, 0x00020000);
    const int kofn = more ? koff : voff, sofn = more ? soff : 0;
    if (it >= fast) lag = 0x7fffffff;
    if ((PT & 6) && it == PTI) px = __builtin_amdgcn_raw_buffer_load_b32(pr, voff, 0, 16);
    const unsigned now = PACE ? (unsigned)__builtin_amdgcn_s_memrealtime() : 0u;
    __builtin_amdgcn_sched_barrier(0);
    auto refill = [&](int p) {
      switch (p) {
        case 0: throttle<0>(lag); break; case 1: throttle<1>(lag); break; case 2: throttle<2>(lag); break; case 3: throttle<3>(lag); break;
        case 4: throttle<4>(lag); break; case 5: throttle<5>(lag); break; case 6: throttle<6>(lag); break; default: throttle<7>(lag); break;
      }
      fill(p, kn, vn, kofn, sofn);
      __builtin_amdgcn_sched_barrier(0);
    };
    f32x4 dd[4];
#pragma unroll
    for (int i = 0; i < 4; ++i) {
      dd[i] = __builtin_amdgcn_mfma_f32_16x16x32_bf16(__builtin_bit_cast(bf16x8, R[i]), __builtin_bit_cast(bf16x8, qf), f32x4{0.f, 0.f, 0.f, 0.f}, 0, 0, 0);
      asm volatile("" :: "v"(dd[i]) : "memory");
      refill(i);
      if (PACE && i == 0 && it > 0) {
        const unsigned mine = now - ((unsigned)(it * tpt) >> 3);
        const int step = (int)(mine - prev);
        ref += step > JUMP ? step : 0;
        prev = mine;
        int lg = (int)(mine - ref);
        if (lg < -LEAD) { ref = mine + LEAD; lg = -LEAD; }
        asm volatile("" : "+s"(lg));
        lg = lg < LAGCAP ? lg : LAGCAP;
        lag = it >= fast ? 0x7fffffff : lg;
        __builtin_amdgcn_sched_barrier(0);
      }
    }
    float sc[4];
#pragma unroll
    for (int i = 0; i < 4; ++i) sc[i] = (qbc<0x00>(dd[i][0]) + qbc<0x55>(dd[i][1])) + (qbc<0xaa>(dd[i][2]) + qbc<0xff>(dd[i][3]));
    const float lmax = fmaxf(fmaxf(sc[0], sc[1]), fmaxf(sc[2], sc[3]));
    if (__any(lmax > m)) {
      const float mn = fmaxf(m, xrow_max(lmax));
      const float alpha = __builtin_amdgcn_exp2f((m - mn) * C2);
      l *= alpha;
#pragma unroll
      for (int e = 0; e < 4; ++e) { acc0[e] *= alpha; acc1[e] *= alpha; }
      m = mn;
    }
    const float mc = m * C2;
    u32x2 bop[4];
#pragma unroll
    for (int r = 0; r < 4; ++r) {
      const float p = __builtin_amdgcn_exp2f(fmaf(sc[r], C2, -mc));
      l += p;
      const unsigned pb = cvtpk(p, p);
      bop[r] = u32x2{pb & mask0, pb & mask1};
    }
    __builtin_amdgcn_sched_barrier(0);
#pragma unroll
    for (int r = 0; r < 4; ++r) {
      const u32x4 vv = R[4 + r];
      const u32x2 a0 = {vv[0], vv[1]}, a1 = {vv[2], vv[3]};
      acc0 = __builtin_amdgcn_mfma_f32_16x16x16bf16_1k(__builtin_bit_cast(s16x4, a0), __builtin_bit_cast(s16x4, bop[r]), acc0, 0, 0, 0);
      acc1 = __builtin_amdgcn_mfma_f32_16x16x16bf16_1k(__builtin_bit_cast(s16x4, a1), __builtin_bit_cast(s16x4, bop[r]), acc1, 0, 0, 0);
      asm volatile("" :: "v"(acc0), "v"(acc1) : "memory");
      refill(4 + r);
    }
  }
  l = xrow_sum(l);
  asm volatile("" :: "v"(px));
  {
    float* rec = lds + (wave * G + (lo >> 2)) * REC;
#pragma unroll
    for (int e = 0; e < 4; ++e) { rec[4 + 8 * (4 * hi + e) + jj] = acc0[e]; rec[4 + 8 * (4 * hi + e) + 4 + jj] = acc1[e]; }
    if (hi == 0 && jj == 0) { rec[0] = m; rec[1] = l; }
  }
  asm volatile("s_waitcnt lgkmcnt(0)\n\ts_barrier" ::: "memory");
  const int g = wave;
  const unsigned tag = cw + 0x9E3779B9u;
  float M = -INFINITY, L = 0.f, o0 = 0.f, o1 = 0.f;
#pragma unroll
  for (int w = 0; w < WAVES; ++w) M = fmaxf(M, lds[(w * G + g) * REC]);
#pragma unroll
  for (int w = 0; w < WAVES; ++w) {
    const float* rec = lds + (w * G + g) * REC;
    const float wt = __builtin_amdgcn_exp2f((rec[0] - M) * C2);
    const float2 a = *reinterpret_cast<const float2*>(rec + 4 + 2 * lane);
    L = fmaf(wt, rec[1], L); o0 = fmaf(wt, a.x, o0); o1 = fmaf(wt, a.y, o1);
  }
  if (!own) {
    __builtin_amdgcn_raw_buffer_store_b128(u32x4{__float_as_uint(o0), tag, __float_as_uint(o1), tag}, gr, voff, rank * 1024, 16);
    __builtin_amdgcn_raw_buffer_store_b128(u32x4{__float_as_uint(M), tag, __float_as_uint(L), tag}, gr, lane == 0 ? 3072 : HREC, rank * 128, 16);
    return;
  }
  auto fresh = [&](int b) {
    unsigned x = (lane & 7) || lane >= 24 ? 0u : (R[b + 3][1] ^ tag) | (R[b + 3][3] ^ tag);
#pragma unroll
    for (int i = 0; i < 3; ++i) x |= (R[b + i][1] ^ tag) | (R[b + i][3] ^ tag);
    return __all(x == 0);
  };
  if (!fresh(0)) {
    int spins = 0;
    while (!fresh(4) && ++spins < (1 << 20)) {
      asm volatile("" ::: "memory");
#pragma unroll
      for (int i = 4; i < 8; ++i) R[i] = __builtin_amdgcn_raw_buffer_load_b128(gr, voff + (i - 4) * 1024, 0, 16);
    }
#pragma unroll
    for (int i = 0; i < 4; ++i) R[i] = R[4 + i];
  }
  float Mi[3], Li[3], Mx = M;
#pragma unroll
  for (int i = 0; i < 3; ++i) {
    Mi[i] = __uint_as_float(__builtin_amdgcn_readlane(R[3][0], 8 * i));
    Li[i] = __uint_as_float(__builtin_amdgcn_readlane(R[3][2], 8 * i));
    Mx = fmaxf(Mx, Mi[i]);
  }
  const float w0 = __builtin_amdgcn_exp2f((M - Mx) * C2);
  L *= w0; o0 *= w0; o1 *= w0;
#pragma unroll
  for (int i = 0; i < 3; ++i) {
    const float wt = __builtin_amdgcn_exp2f((Mi[i] - Mx) * C2);
    L = fmaf(wt, Li[i], L);
    o0 = fmaf(wt, __uint_as_float(R[i][0]), o0); o1 = fmaf(wt, __uint_as_float(R[i][2]), o1);
  }
  const float inv = 1.f / L;
  out[(bh * G + g) * (D / 2) + lane] = cvtpk(o0 * inv, o1 * inv);
  if (g == 0 && lane == 0) __hip_atomic_store(ctl, tag, RLX_AGENT);
  asm volatile("" :: "v"(R[4]), "v"(R[5]), "v"(R[6]), "v"(R[7]));
}

extern "C" void kernel_launch(void* const* d_in, const int* in_sizes, int n_in, void* d_out, int out_size, void* d_ws, size_t ws_size,
                              hipStream_t stream) {
  hipLaunchKernelGGL(attn_split, dim3(GRIDN), dim3(NTHR), 0, stream, (const unsigned char*)d_in[0], (const unsigned char*)d_in[1],
                     (const unsigned char*)d_in[2], (unsigned char*)d_ws, (unsigned*)d_out);
}
```

```cpp
#include <hip/hip_runtime.h>
#include <hip/hip_bf16.h>
#include <cstdint>

#define NSPLIT 4
#define WAVES 4
#define MAP 0
#define KAUX 18
#define VAUX 18

#define PACE 1
#define NC8 32
#define GAIN 56
#define NMIN 2
#define TPT8 852
#define OFF 90
#define SPRINT 29
#define SKEW 0
#define LEAD 40
#define JUMP 160
#define LAGCAP 1000
#define PT 0
#define PTI 30
#define EQ1 0
#define G8 0
#define XOFF 0
#define J0 1000
#define DRY 0
#define LOOK3 1
#define LP 2
#define LOOKC 0
#define CB 4
#define GRIDN (B * HKV * NSPLIT)
constexpr int B = 8, HQ = 32, HKV = 8, G = 4, SK = 8192, D = 128;
constexpr int NTHR = (WAVES + DRY) * 64;
constexpr int TOK_PER_WG = SK / NSPLIT;
constexpr int TILES_PER_WAVE = TOK_PER_WG / (16 * WAVES);
constexpr float C2 = 0.088388347648318440f * 1.4426950408889634f;

using u32x4 = __attribute__((ext_vector_type(4))) unsigned;
using u32x2 = __attribute__((ext_vector_type(2))) unsigned;
using f32x4 = __attribute__((ext_vector_type(4))) float;
using bf16x8 = __attribute__((ext_vector_type(8))) __bf16;
using f32x2 = __attribute__((ext_vector_type(2))) float;
using bf16x2 = __attribute__((ext_vector_type(2))) __bf16;
using s16x4 = __attribute__((ext_vector_type(4))) short;

__device__ __forceinline__ unsigned cvtpk(float lo, float hi) {
  const f32x2 f = {lo, hi};
  return __builtin_bit_cast(unsigned, __builtin_convertvector(f, bf16x2));
}
__device__ __forceinline__ float xrow_max(float x) {
  auto s = __builtin_amdgcn_permlane16_swap(__float_as_uint(x), __float_as_uint(x), false, false);
  x = fmaxf(__uint_as_float(s[0]), __uint_as_float(s[1]));
  auto t = __builtin_amdgcn_permlane32_swap(__float_as_uint(x), __float_as_uint(x), false, false);
  return fmaxf(__uint_as_float(t[0]), __uint_as_float(t[1]));
}
__device__ __forceinline__ float xrow_sum(float x) {
  auto s = __builtin_amdgcn_permlane16_swap(__float_as_uint(x), __float_as_uint(x), false, false);
  x = __uint_as_float(s[0]) + __uint_as_float(s[1]);
  auto t = __builtin_amdgcn_permlane32_swap(__float_as_uint(x), __float_as_uint(x), false, false);
  return __uint_as_float(t[0]) + __uint_as_float(t[1]);
}

template <int CTRL>
__device__ __forceinline__ float qbc(float x) {
  return __uint_as_float(__builtin_amdgcn_mov_dpp(__float_as_uint(x), CTRL, 0xf, 0xf, true));
}

template <int P>
__device__ __forceinline__ void throttle(int lag) {
  constexpr int ph = ((P & 1) << 2) | (P & 2) | ((P & 4) >> 2);
#define THR(k) ((8 * (k) - NC8 - ph) * GAIN / 8)
  asm volatile("s_cmp_ge_i32 %0, %1\n\ts_cbranch_scc1 1f\n\ts_waitcnt vmcnt(6)\n\t"
               "s_cmp_ge_i32 %0, %2\n\ts_cbranch_scc1 1f\n\ts_waitcnt vmcnt(5)\n\t"
               "s_cmp_ge_i32 %0, %3\n\ts_cbranch_scc1 1f\n\ts_waitcnt vmcnt(4)\n\t"
               "s_cmp_ge_i32 %0, %4\n\ts_cbranch_scc1 1f\n\ts_waitcnt vmcnt(3)\n\t"
               "s_cmp_ge_i32 %0, %5\n\ts_cbranch_scc1 1f\n\ts_waitcnt vmcnt(2)\n\t"
               "1:" :: "s"(lag), "i"(THR(7)), "i"(THR(6)), "i"(THR(5)), "i"(THR(4)), "i"(THR(3)) : "scc", "memory");
#undef THR
}

constexpr int REC = 4 + D;

typedef __attribute__((address_space(1))) unsigned gu32;
#define RLX_AGENT __ATOMIC_RELAXED, __HIP_MEMORY_SCOPE_AGENT
constexpr int CTL_STRIDE = 256;
constexpr int CTL_BYTES = B * HKV * CTL_STRIDE;
constexpr int HREC = 4096;
constexpr int HUSED = 3072 + 3 * 128;
constexpr int GRP_BYTES = G * HREC;
constexpr int NT = TILES_PER_WAVE;
constexpr int T0 = EQ1 ? NT / 2 : 0;
constexpr int TPT_N = TPT8 * (3 * (NT - 1 - T0) + SPRINT - T0) / (4 * (NT - 1 - T0));
constexpr int TPT_O = (TPT_N * (NT - 1 - T0) - 8 * SKEW) / (SPRINT - T0);
static_assert(NSPLIT == 4 && WAVES == G, "layout");

__global__ void __launch_bounds__(NTHR, 2 + DRY) attn_split(const unsigned char* __restrict__ q, const unsigned char* __restrict__ k,
                                                   const unsigned char* __restrict__ v, unsigned char* ws, unsigned* __restrict__ out) {
  __shared__ __attribute__((aligned(16))) float lds[(WAVES + DRY) * G * REC];
  __shared__ f32x4 sbuf[(WAVES + DRY) * NT * 16];
  const int bid = MAP == 0 ? blockIdx.x : (blockIdx.x & 7) * (GRIDN / 8) + (blockIdx.x >> 3);
  const int bh = (unsigned)bid / NSPLIT, s = (unsigned)bid % NSPLIT;
  const int tid = threadIdx.x, lane = tid & 63, wave = __builtin_amdgcn_readfirstlane(tid >> 6);
  const int hi = lane >> 4, lo = lane & 15;
  constexpr int SLICE = (32 * 4096) << CB, KVB = B * HKV * SK * D * 2;
  const unsigned W = bid * WAVES + wave;
  int koff = (lo >> 2) * 256 + hi * 64 + (lo & 3) * 16;
  int voff = lane * 16;
  constexpr int TSTRIDE = G8 ? 4096 : 4096 << CB, TRIP = 8192 << CB;
  const int sbase = (W >> CB) * SLICE + (W & ((1 << CB) - 1)) * (G8 ? 8192 : 4096);
  int soff = sbase;
  int qoff = bh * (G * D * 2);
  const bool dry = DRY && wave == WAVES;
  int kvb = dry ? 0 : KVB;
  asm volatile("" : "+v"(koff), "+v"(voff), "+s"(soff), "+s"(qoff), "+s"(kvb));
  __builtin_amdgcn_sched_barrier(0);
  void* const kb = (void*)k;
  void* const vb = (void*)v;
  const auto kr = __builtin_amdgcn_make_buffer_rsrc(kb, 0, kvb, 0x00020000);
  u32x4 R[8];
  auto fill = [&](int i, decltype(kr) rs, int vo, int s0, int s1) {
    R[i] = __builtin_amdgcn_raw_buffer_load_b128(rs, vo + (i & 3) * 1024, i < 4 ? s0 : s1, KAUX);
  };
  const u32x4 qf = __builtin_amdgcn_raw_buffer_load_b128(__builtin_amdgcn_make_buffer_rsrc((void*)q, 0, B * HQ * D * 2, 0x00020000), koff, qoff, 0);
  __builtin_amdgcn_sched_barrier(0);
#pragma unroll
  for (int i = 0; i < 8; ++i) fill(i, kr, koff, soff, soff + TSTRIDE);
  __builtin_amdgcn_sched_barrier(0);
  gu32* const ctl = (gu32*)(ws + bh * CTL_STRIDE);
  unsigned cw = __builtin_amdgcn_raw_buffer_load_b32(__builtin_amdgcn_make_buffer_rsrc((void*)ctl, 0, 4, 0x00020000), 0, 0, 16);
  __builtin_amdgcn_sched_barrier(0);
  const unsigned t_beg = (unsigned)__builtin_amdgcn_s_memrealtime();
  const int jj = lo & 3;
  const unsigned mask0 = jj == 0 ? 0x0000ffffu : (jj == 1 ? 0xffff0000u : 0u);
  const unsigned mask1 = jj == 2 ? 0x0000ffffu : (jj == 3 ? 0xffff0000u : 0u);

  const int rank = (s - (bh >> 1) - 1) & 3;
  const bool own = rank == 3;
  void* const gb = (void*)(ws + CTL_BYTES + (size_t)bh * GRP_BYTES + wave * HREC);
  const auto gr = __builtin_amdgcn_make_buffer_rsrc(gb, 0, dry ? 0 : HUSED, 0x00020000);
  const int grec = own && !dry ? HUSED : 0;
  const auto pr = __builtin_amdgcn_make_buffer_rsrc((void*)(out + bh * G * (D / 2)), 0, own && !dry ? G * D * 2 : 0, 0x00020000);
  const unsigned px = __builtin_amdgcn_raw_buffer_load_b32(pr, lane << 12, 0, 16);

  const int fast = own ? SPRINT : NT - 1;
  int tpt = EQ1 ? TPT8 : own ? TPT_O : TPT_N, due = 0;
  unsigned ref = t_beg + OFF - ((blockIdx.x & 4) ? XOFF : 0), prev = ref + J0;
  int lag = 0;
  auto pace = [&](int it, unsigned long long now) {
    asm volatile("" : "+s"(now));
    const unsigned mine = (unsigned)now - ((unsigned)due >> 3);
    const int step = (int)(mine - prev);
    ref += step > JUMP ? step : 0;
    prev = mine;
    int lg = (int)(mine - ref);
    if (lg < -LEAD) { ref = mine + LEAD; lg = -LEAD; }
    asm volatile("" : "+s"(lg));
    lg = lg < LAGCAP ? lg : LAGCAP;
    lag = it >= fast ? 0x7fffffff : lg;
    __builtin_amdgcn_sched_barrier(0);
  };
  auto refill = [&](int p, decltype(kr) rs, int vo, int s0, int s1) {
    switch (p) {
      case 0: throttle<0>(lag); break; case 1: throttle<1>(lag); break; case 2: throttle<2>(lag); break; case 3: throttle<3>(lag); break;
      case 4: throttle<4>(lag); break; case 5: throttle<5>(lag); break; case 6: throttle<6>(lag); break; default: throttle<7>(lag); break;
    }
    fill(p, rs, vo, s0, s1);
    __builtin_amdgcn_sched_barrier(0);
  };
  f32x4* const sw = sbuf + wave * (NT * 16) + (lane >> 2);
  float m = -INFINITY;
  int skip = dry ? NT / 2 - 1 : 0;
  asm volatile("" : "+s"(skip));
  __builtin_assume(skip < NT / 2);
#pragma unroll 1
  for (int it = skip; it < NT / 2; ++it) {
    const bool more = it + 1 < NT / 2;
    soff = more ? soff + TRIP : sbase;
    const auto rn = __builtin_amdgcn_make_buffer_rsrc(more ? kb : vb, 0, kvb, 0x00020000);
    const int vo = more ? koff : voff;
    const unsigned long long now = __builtin_amdgcn_s_memrealtime();
    __builtin_amdgcn_sched_barrier(0);
#pragma unroll
    for (int h = 0; h < 2; ++h) {
      f32x4 dd[4];
#pragma unroll
      for (int i = 0; i < 4; ++i) {
        dd[i] = __builtin_amdgcn_mfma_f32_16x16x32_bf16(__builtin_bit_cast(bf16x8, R[4 * h + i]), __builtin_bit_cast(bf16x8, qf), f32x4{0.f, 0.f, 0.f, 0.f}, 0, 0, 0);
        asm volatile("" :: "v"(dd[i]) : "memory");
        refill(4 * h + i, rn, vo, soff, soff + TSTRIDE);
        if (h == 0 && i == 0 && it > 0) pace(it, now);
      }
      f32x4 sc;
#pragma unroll
      for (int i = 0; i < 4; ++i) sc[i] = (qbc<0x00>(dd[i][0]) + qbc<0x55>(dd[i][1])) + (qbc<0xaa>(dd[i][2]) + qbc<0xff>(dd[i][3]));
      m = fmaxf(fmaxf(m, fmaxf(sc[0], sc[1])), fmaxf(sc[2], sc[3]));
      sw[(2 * it + h) * 16] = sc;
      __builtin_amdgcn_sched_barrier(0);
    }
    due += tpt;
  }
  m = xrow_max(m);
  if (EQ1) tpt = own ? TPT_O : TPT_N;
  const float mc = m * C2;
  float l = 0.f, ps;
  u32x2 bop[2][4];
  f32x4 sn = sw[0];
  auto weigh = [&](int h, int next) {
    ps = 0.f;
#pragma unroll
    for (int r = 0; r < 4; ++r) {
      const float p = __builtin_amdgcn_exp2f(fmaf(sn[r], C2, -mc));
      ps += p;
      const unsigned pb = cvtpk(p, p);
      bop[h][r] = u32x2{pb & mask0, pb & mask1};
    }
    sn = sw[(next & (NT - 1)) * 16];
    __builtin_amdgcn_sched_barrier(0);
  };
  weigh(0, 1);
  f32x4 acc0 = {0.f, 0.f, 0.f, 0.f}, acc1 = {0.f, 0.f, 0.f, 0.f};
  u32x4 A[4], Cc[4], T[4];
  auto look = [&](u32x4* X) {
#pragma unroll
    for (int i = 0; i < 4; ++i) X[i] = __builtin_amdgcn_raw_buffer_load_b128(__builtin_amdgcn_make_buffer_rsrc(gb, 0, grec, 0x00020000), voff + i * 1024, 0, 16);
    __builtin_amdgcn_sched_barrier(0);
  };
  auto pv = [&](int h, int r) {
    const u32x4 vv = R[4 * h + r];
    const u32x2 a0 = {vv[0], vv[1]}, a1 = {vv[2], vv[3]};
    acc0 = __builtin_amdgcn_mfma_f32_16x16x16bf16_1k(__builtin_bit_cast(s16x4, a0), __builtin_bit_cast(s16x4, bop[h][r]), acc0, 0, 0, 0);
    acc1 = __builtin_amdgcn_mfma_f32_16x16x16bf16_1k(__builtin_bit_cast(s16x4, a1), __builtin_bit_cast(s16x4, bop[h][r]), acc1, 0, 0, 0);
    asm volatile("" :: "v"(acc0), "v"(acc1) : "memory");
  };
  const auto rn = __builtin_amdgcn_make_buffer_rsrc(vb, 0, kvb, 0x00020000);
#pragma unroll 1
  for (int it = NT / 2 + (skip & ~1); it < NT - 1; ++it) {
    soff += TRIP;
    const int s0 = soff, s1 = soff + TSTRIDE;
    if (it >= fast) lag = 0x7fffffff;
    const unsigned long long now = __builtin_amdgcn_s_memrealtime();
    __builtin_amdgcn_sched_barrier(0);
#pragma unroll
    for (int h = 0; h < 2; ++h) {
      l += ps;
#pragma unroll
      for (int r = 0; r < 4; ++r) {
        pv(h, r);
        refill(4 * h + r, rn, voff, s0, s1);
        if (h == 0 && r == 0) pace(it, now);
        if (r == 1) weigh(h ^ 1, 2 * it + h + 2);
      }
    }
    due += tpt;
  }
  if (LP == 0) look(A);
  l += ps;
  weigh(1, 0);
  l = xrow_sum(l + ps);
  float* const rec = lds + (wave * G + (lo >> 2)) * REC;
  if (hi == 0 && jj == 0) { rec[0] = m; rec[1] = l; }
  __builtin_amdgcn_sched_barrier(0);
#pragma unroll
  for (int p = 0; p < 8; ++p) {
    pv(p >> 2, p & 3);
    __builtin_amdgcn_sched_barrier(0);
    if (p + 1 == LP) look(A);
    if (p + 1 == LOOKC) look(Cc);
  }
  asm volatile("" :: "v"(px));
  {
#pragma unroll
    for (int e = 0; e < 4; ++e) { rec[4 + 8 * (4 * hi + e) + jj] = acc0[e]; rec[4 + 8 * (4 * hi + e) + 4 + jj] = acc1[e]; }
  }
  asm volatile("s_waitcnt lgkmcnt(0)\n\ts_cmp_eq_u32 %0, 0\n\ts_cbranch_scc1 1f\n\ts_barrier\n1:" :: "s"(kvb) : "scc", "memory");
  const int g = wave;
  const unsigned tag = cw + 0x9E3779B9u;
  float M = -INFINITY, L = 0.f, o0 = 0.f, o1 = 0.f;
  float2 ml[WAVES], oa[WAVES];
#pragma unroll
  for (int w = 0; w < WAVES; ++w) {
    const float* rw = lds + (w * G + g) * REC;
    ml[w] = *reinterpret_cast<const float2*>(rw);
    oa[w] = *reinterpret_cast<const float2*>(rw + 4 + 2 * lane);
  }
  __builtin_amdgcn_sched_barrier(0);
  if (LOOK3) look(T); else { T[0] = T[1] = T[2] = T[3] = u32x4{tag + 1, tag + 1, tag + 1, tag + 1}; }
  asm volatile("" : "+v"(ml[0].x), "+v"(ml[1].x), "+v"(ml[2].x), "+v"(ml[3].x) :: "memory");
#pragma unroll
  for (int w = 0; w < WAVES; ++w) M = fmaxf(M, ml[w].x);
#pragma unroll
  for (int w = 0; w < WAVES; ++w) {
    const float wt = __builtin_amdgcn_exp2f((ml[w].x - M) * C2);
    L = fmaf(wt, ml[w].y, L); o0 = fmaf(wt, oa[w].x, o0); o1 = fmaf(wt, oa[w].y, o1);
  }
  if (!own) {
    __builtin_amdgcn_raw_buffer_store_b128(u32x4{__float_as_uint(o0), tag, __float_as_uint(o1), tag}, gr, voff, rank * 1024, 16);
    __builtin_amdgcn_raw_buffer_store_b128(u32x4{__float_as_uint(M), tag, __float_as_uint(L), tag}, gr, lane == 0 ? 3072 : HREC, rank * 128, 16);
    return;
  }
  auto fresh = [&](const u32x4* X) {
    unsigned x = (lane & 7) || lane >= 24 ? 0u : (X[3][1] ^ tag) | (X[3][3] ^ tag);
#pragma unroll
    for (int i = 0; i < 3; ++i) x |= (X[i][1] ^ tag) | (X[i][3] ^ tag);
    return __all(x == 0);
  };
#pragma unroll
  for (int i = 0; i < 4; ++i) R[i] = A[i];
  if (!fresh(R)) {
    bool second = false;
    if (LOOKC) {
      second = fresh(Cc);
#pragma unroll
      for (int i = 0; i < 4; ++i) R[i] = Cc[i];
    }
    if (!second) {
#pragma unroll
      for (int i = 0; i < 4; ++i) { R[i] = T[i]; asm volatile("" : "+v"(R[i])); }
      int spins = dry ? 2 : 1 << 20;
      while (!fresh(R) && --spins) {
        asm volatile("" ::: "memory");
        look(R);
        asm volatile("" : "+v"(R[0]), "+v"(R[1]), "+v"(R[2]), "+v"(R[3]));
      }
    }
  }
  float Mi[3], Li[3], Mx = M;
#pragma unroll
  for (int i = 0; i < 3; ++i) {
    Mi[i] = __uint_as_float(__builtin_amdgcn_readlane(R[3][0], 8 * i));
    Li[i] = __uint_as_float(__builtin_amdgcn_readlane(R[3][2], 8 * i));
    Mx = fmaxf(Mx, Mi[i]);
  }
  const float w0 = __builtin_amdgcn_exp2f((M - Mx) * C2);
  L *= w0; o0 *= w0; o1 *= w0;
#pragma unroll
  for (int i = 0; i < 3; ++i) {
    const float wt = __builtin_amdgcn_exp2f((Mi[i] - Mx) * C2);
    L = fmaf(wt, Li[i], L);
    o0 = fmaf(wt, __uint_as_float(R[i][0]), o0); o1 = fmaf(wt, __uint_as_float(R[i][2]), o1);
  }
  const float inv = 1.f / L;
  __builtin_amdgcn_raw_buffer_store_b32(cvtpk(o0 * inv, o1 * inv), pr, lane * 4, g * (D * 2), 0);
  if (g == 0 && lane == 0) __hip_atomic_store(ctl, tag, RLX_AGENT);
  if (LOOKC) asm volatile("" :: "v"(Cc[0]), "v"(Cc[1]), "v"(Cc[2]), "v"(Cc[3]));
  asm volatile("" :: "v"(T[0]), "v"(T[1]), "v"(T[2]), "v"(T[3]));
}

extern "C" void kernel_launch(void* const* d_in, const int* in_sizes, int n_in, void* d_out, int out_size, void* d_ws, size_t ws_size,
                              hipStream_t stream) {
  hipLaunchKernelGGL(attn_split, dim3(GRIDN), dim3(NTHR), 0, stream, (const unsigned char*)d_in[0], (const unsigned char*)d_in[1],
                     (const unsigned char*)d_in[2], (unsigned char*)d_ws, (unsigned*)d_out);
}
```
